```python
import torch

def _softmax(x, axis=-1):
    return torch.softmax(x, dim=axis)
def _gelu(x):
    return torch.nn.functional.gelu(x, approximate="tanh")
def _silu(x):
    return torch.nn.functional.silu(x)
def _sigmoid(x):
    return torch.sigmoid(x)
def _layernorm(x, axis=-1, eps=1e-5):
    m = x.mean(dim=axis, keepdim=True)
    v = x.var(dim=axis, keepdim=True, unbiased=False)
    return (x - m) / torch.sqrt(v + eps)
def _scatter_add(tgt, idx, src, axis=0):
    return tgt.clone().index_add_(axis, idx, src)
def _segment_sum(src, seg, n):
    out = torch.zeros((n,) + src.shape[1:], dtype=src.dtype)
    return out.index_add_(0, seg, src)
def _pad(x, pw):
    flat = tuple(v for pair in reversed(pw) for v in pair)
    return torch.nn.functional.pad(x, flat)

kernel_name = 'gated_mix__sliding_block_cached__geglu_ffn__decode'

def setup_inputs():

    _w = (lambda a: (lambda t: t.to(torch.bfloat16) if t.is_floating_point() else t)(torch.as_tensor(a)))
    def _A_setup():
        B, Sq, D = 8, 1, 640
        Hq, Hkv, Sk, Dh = 32, 32, 4096, 192
        assert Hq % Hkv == 0, (Hq, Hkv)
        return {
            "x": _w((lambda *s: torch.randn(*s, dtype=torch.bfloat16) * 0.05)(B, Sq, D)),
            "k_cache": _w((lambda *s: torch.randn(*s, dtype=torch.bfloat16) * 6.4)(B, Hkv, Sk, Dh)),
            "v_cache": _w((lambda *s: torch.randn(*s, dtype=torch.bfloat16) * 0.2)(B, Hkv, Sk, Dh)),
            "wq": _w((lambda *s: torch.randn(*s, dtype=torch.bfloat16) * 6.4)(D, Hq * Dh) * (D ** -0.5)),
            "wo": _w((lambda *s: torch.randn(*s, dtype=torch.bfloat16) * 0.2)(Hq * Dh, D) * ((Hq * Dh) ** -0.5)),
        }
    _A_in = _A_setup()

    def _B_setup():
        B, S, D, F = 8, 1, 640, 1536
        return {
            "x": _w((lambda *s: torch.randn(*s, dtype=torch.bfloat16) * 0.05)(B, S, D)),
            "w_gate": _w((lambda *s: torch.randn(*s, dtype=torch.bfloat16) * 0.05)(D, F)),
            "w_up": _w((lambda *s: torch.randn(*s, dtype=torch.bfloat16) * 0.05)(D, F)),
            "w_down": _w((lambda *s: torch.randn(*s, dtype=torch.bfloat16) * 0.05)(F, D)),
        }
    _B_in = _B_setup()

    return {
        "A_x": _A_in["x"],
        "A_k_cache": _A_in["k_cache"],
        "A_v_cache": _A_in["v_cache"],
        "A_wq": _A_in["wq"],
        "A_wo": _A_in["wo"],
        "B_x": _B_in["x"],
        "B_w_gate": _B_in["w_gate"],
        "B_w_up": _B_in["w_up"],
        "B_w_down": _B_in["w_down"],
        "gate_w": _w((lambda *s: torch.randn(*s, dtype=torch.bfloat16) * 0.05)(640) * 16.0),
    }

def reference(A_x, A_k_cache, A_v_cache, A_wq, A_wo, B_x, B_w_gate, B_w_up, B_w_down, gate_w):
    def _A_ref(x, k_cache, v_cache, wq, wo):
        B, Sq, D = x.shape
        Hq, Dh = 32, 192
        Hkv, Sk = k_cache.shape[1], k_cache.shape[2]
        W = min(4096, max(256, Sk // 4))
        rep = Hq // Hkv
        q = (lambda x, *d: x.permute(*d))((x @ wq).reshape(B, Sq, Hq, Dh), 0, 2, 1, 3)
        k = (lambda x, n, axis: torch.repeat_interleave(x, n, dim=axis))(k_cache[:, :, Sk - W:, :], rep, axis=1)
        v = (lambda x, n, axis: torch.repeat_interleave(x, n, dim=axis))(v_cache[:, :, Sk - W:, :], rep, axis=1)
        scores = torch.einsum("bhqd,bhkd->bhqk", q, k) * (Dh ** -0.5)
        p = _softmax(scores, axis=-1)
        o = torch.einsum("bhqk,bhkd->bhqd", p, v)
        return (lambda x, *d: x.permute(*d))(o, 0, 2, 1, 3).reshape(B, Sq, Hq * Dh) @ wo

    def _B_ref(x, w_gate, w_up, w_down):
        return (_gelu(x @ w_gate) * (x @ w_up)) @ w_down

    x = A_x
    g = _sigmoid((x * gate_w).sum(-1, keepdims=True))
    a = _A_ref(x, A_k_cache, A_v_cache, A_wq, A_wo)
    b = _B_ref(x, B_w_gate, B_w_up, B_w_down)
    return g * a + (1 - g) * b
```

```cpp
#include <hip/hip_runtime.h>
#include <hip/hip_bf16.h>
#include <stdint.h>


typedef unsigned int u32;
using u32x4 = __attribute__((ext_vector_type(4))) unsigned;
using f32x4 = __attribute__((ext_vector_type(4))) float;
using bf16x2 = __attribute__((ext_vector_type(2))) __bf16;

constexpr int NB = 8, DM = 640, HQ = 32, SK = 4096, DH = 192, WIN = 1024, FF = 1536;
constexpr int NATT = NB * HQ;
constexpr int NFFN = 24;
constexpr int FU = FF / NFFN;
constexpr int NTHR = 512;
constexpr int NWAVE = NTHR / 64;
constexpr int KEYS_PER_WAVE = WIN / NWAVE;
constexpr int NST = KEYS_PER_WAVE / 8;
constexpr int NBUF = 2;

constexpr size_t WS_ATT = 0;
constexpr size_t WS_FFN = (size_t)NATT * DM * 4;
constexpr size_t WS_END = WS_FFN + (size_t)NFFN * NB * DM * 4;

__device__ __forceinline__ float bf_lo(u32 x) { return __builtin_bit_cast(float, x << 16); }
__device__ __forceinline__ float bf_hi(u32 x) { return __builtin_bit_cast(float, x & 0xffff0000u); }
__device__ __forceinline__ float rbf(float x) {
  u32 u = __builtin_bit_cast(u32, x);
  u = (u + 0x7fffu + ((u >> 16) & 1u)) & 0xffff0000u;
  return __builtin_bit_cast(float, u);
}
__device__ __forceinline__ unsigned short f2bf_bits(float x) {
  u32 u = __builtin_bit_cast(u32, x);
  u = (u + 0x7fffu + ((u >> 16) & 1u));
  return (unsigned short)(u >> 16);
}
__device__ __forceinline__ float dot2(u32 k, u32 q, float acc) {
  return __builtin_amdgcn_fdot2_f32_bf16(__builtin_bit_cast(bf16x2, k), __builtin_bit_cast(bf16x2, q), acc, false);
}
template <int CTRL> __device__ __forceinline__ float dppf(float x) {
  return __builtin_bit_cast(float, __builtin_amdgcn_mov_dpp(__builtin_bit_cast(int, x), CTRL, 0xf, 0xf, true));
}

constexpr int LDS_FLOATS = 64 * DH + 1024;

__global__ void __launch_bounds__(NTHR, 2)
fused_main(const unsigned short* __restrict__ x, const unsigned short* __restrict__ kc, const unsigned short* __restrict__ vc,
           const unsigned short* __restrict__ wq, const unsigned short* __restrict__ wo,
           const unsigned short* __restrict__ wg, const unsigned short* __restrict__ wu, const unsigned short* __restrict__ wd,
           float* __restrict__ att_part, float* __restrict__ ffn_part) {
  __shared__ __attribute__((aligned(16))) float lds[LDS_FLOATS];
  const int t = threadIdx.x;
  const int lane = t & 63, wave = t >> 6;

  if (blockIdx.x < NATT) {
    const int bid = blockIdx.x;
    const int xg = bid & 7, r = bid >> 3;
    const int h = (r & 3) * 8 + xg;
    const int b = r >> 2;
    const int rs = lane >> 3, sub = lane & 7;

    const size_t kvbase = ((size_t)(b * HQ + h) * SK + (SK - WIN) + wave * KEYS_PER_WAVE + rs) * DH + sub * 8;
    const u32x4* kp = reinterpret_cast<const u32x4*>(kc + kvbase);
    const u32x4* vp = reinterpret_cast<const u32x4*>(vc + kvbase);

    u32x4 kb[NBUF][3], vb[NBUF][3];
#pragma unroll
    for (int s = 0; s < NBUF; ++s) {
#pragma unroll
      for (int j = 0; j < 3; ++j) {
        kb[s][j] = __builtin_nontemporal_load(kp + s * 192 + j * 8);
        vb[s][j] = __builtin_nontemporal_load(vp + s * 192 + j * 8);
      }
    }

    float* xs = lds;
    float* red = lds + 640;
    unsigned short* qs = reinterpret_cast<unsigned short*>(lds + 640 + 20 * DH);
    for (int i = t; i < DM; i += NTHR) xs[i] = __builtin_bit_cast(float, (u32)x[b * DM + i] << 16);
    __syncthreads();
    if (t < 480) {
      const int col = t % 24, rg = t / 24;
      float a[8];
#pragma unroll
      for (int e = 0; e < 8; ++e) a[e] = 0.f;
      const unsigned short* wrow = wq + (size_t)rg * (HQ * DH) + h * DH + col * 8;
#pragma unroll 8
      for (int it = 0; it < 32; ++it) {
        const u32x4 w = *reinterpret_cast<const u32x4*>(wrow + (size_t)it * 20 * (HQ * DH));
        const float xv = xs[it * 20 + rg];
#pragma unroll
        for (int e = 0; e < 4; ++e) {
          a[2 * e] = fmaf(xv, bf_lo(w[e]), a[2 * e]);
          a[2 * e + 1] = fmaf(xv, bf_hi(w[e]), a[2 * e + 1]);
        }
      }
#pragma unroll
      for (int e = 0; e < 8; ++e) red[rg * DH + col * 8 + e] = a[e];
    }
    __syncthreads();
    if (t < DH) {
      float s = 0.f;
#pragma unroll
      for (int g = 0; g < 20; ++g) s += red[g * DH + t];
      qs[t] = f2bf_bits(s);
    }
    __syncthreads();
    u32x4 qv[3];
#pragma unroll
    for (int j = 0; j < 3; ++j) qv[j] = *reinterpret_cast<const u32x4*>(qs + (sub + 8 * j) * 8);

    const float SC = 0.07216878364870322f * 1.4426950408889634f;
    float m = -INFINITY, l = 0.f;
    float acc[24];
#pragma unroll
    for (int i = 0; i < 24; ++i) acc[i] = 0.f;

#pragma unroll 1
    for (int s0 = 0; s0 < NST; s0 += NBUF) {
#pragma unroll
      for (int cur = 0; cur < NBUF; ++cur) {
        const int s = s0 + cur;
        float sc = 0.f;
#pragma unroll
        for (int j = 0; j < 3; ++j)
#pragma unroll
          for (int w = 0; w < 4; ++w) sc = dot2(kb[cur][j][w], qv[j][w], sc);
        sc += dppf<0xB1>(sc);
        sc += dppf<0x4E>(sc);
        sc += dppf<0x141>(sc);
        sc *= SC;
        const float mn = fmaxf(m, sc);
        const float alpha = __builtin_amdgcn_exp2f(m - mn);
        const float p = __builtin_amdgcn_exp2f(sc - mn);
        l = fmaf(l, alpha, p);
        m = mn;
#pragma unroll
        for (int j = 0; j < 3; ++j)
#pragma unroll
          for (int w = 0; w < 4; ++w) {
            const u32 vv = vb[cur][j][w];
            acc[j * 8 + 2 * w] = fmaf(p, bf_lo(vv), acc[j * 8 + 2 * w] * alpha);
            acc[j * 8 + 2 * w + 1] = fmaf(p, bf_hi(vv), acc[j * 8 + 2 * w + 1] * alpha);
          }
        if (s + NBUF < NST) {
#pragma unroll
          for (int j = 0; j < 3; ++j) {
            kb[cur][j] = __builtin_nontemporal_load(kp + (s + NBUF) * 192 + j * 8);
            vb[cur][j] = __builtin_nontemporal_load(vp + (s + NBUF) * 192 + j * 8);
          }
        }
      }
    }

    float* mlm = lds + 64 * DH;
    float* mll = mlm + 64;
    float* buf = lds;
    const int slot = wave * 8 + rs;
    __syncthreads();
    if (sub == 0) { mlm[slot] = m; mll[slot] = l; }
    __syncthreads();
    float M = -INFINITY;
#pragma unroll 8
    for (int i = 0; i < 64; ++i) M = fmaxf(M, mlm[i]);
    float L = 0.f;
#pragma unroll 8
    for (int i = 0; i < 64; ++i) L += mll[i] * __builtin_amdgcn_exp2f(mlm[i] - M);
    const float msc = __builtin_amdgcn_exp2f(m - M);
#pragma unroll
    for (int j = 0; j < 3; ++j) {
      f32x4 o0, o1;
#pragma unroll
      for (int e = 0; e < 4; ++e) { o0[e] = acc[j * 8 + e] * msc; o1[e] = acc[j * 8 + 4 + e] * msc; }
      float* dst = buf + slot * DH + (sub + 8 * j) * 8;
      *reinterpret_cast<f32x4*>(dst) = o0;
      *reinterpret_cast<f32x4*>(dst + 4) = o1;
    }
    __syncthreads();
    float ov = 0.f;
    if (t < DH) {
#pragma unroll 8
      for (int i = 0; i < 64; ++i) ov += buf[i * DH + t];
      ov = rbf(ov / L);
    }
    __syncthreads();
    float* os = lds;
    float* red2 = lds + 256;
    if (t < DH) os[t] = ov;
    __syncthreads();

    if (t < 480) {
      const int cc = t % 80, rg = t / 80;
      float a[8];
#pragma unroll
      for (int e = 0; e < 8; ++e) a[e] = 0.f;
      const unsigned short* wrow = wo + (size_t)(h * DH + rg * 32) * DM + cc * 8;
#pragma unroll 8
      for (int i = 0; i < 32; ++i) {
        const u32x4 w = *reinterpret_cast<const u32x4*>(wrow + (size_t)i * DM);
        const float o = os[rg * 32 + i];
#pragma unroll
        for (int e = 0; e < 4; ++e) {
          a[2 * e] = fmaf(o, bf_lo(w[e]), a[2 * e]);
          a[2 * e + 1] = fmaf(o, bf_hi(w[e]), a[2 * e + 1]);
        }
      }
#pragma unroll
      for (int e = 0; e < 8; ++e) red2[rg * DM + cc * 8 + e] = a[e];
    }
    __syncthreads();
    for (int n = t; n < DM; n += NTHR) {
      float s = 0.f;
#pragma unroll
      for (int g = 0; g < 6; ++g) s += red2[g * DM + n];
      att_part[(size_t)(b * HQ + h) * DM + n] = s;
    }
  } else {
    const int c = blockIdx.x - NATT;
    float* xs = lds;
    float* red = lds + NB * DM;
    float* hs = red + 2 * 4 * 8 * 64;
    for (int i = t; i < NB * DM; i += NTHR) xs[i] = __builtin_bit_cast(float, (u32)x[i] << 16);
    __syncthreads();
    {
      const int mat = t >> 8, tt = t & 255, cc = tt & 7, rg = tt >> 3;
      const unsigned short* W = (mat ? wu : wg) + (size_t)c * FU + cc * 8;
      float a[8][8];
#pragma unroll
      for (int bb = 0; bb < 8; ++bb)
#pragma unroll
        for (int e = 0; e < 8; ++e) a[bb][e] = 0.f;
#pragma unroll 4
      for (int i = 0; i < 20; ++i) {
        const int row = rg + 32 * i;
        const u32x4 w = *reinterpret_cast<const u32x4*>(W + (size_t)row * FF);
        float wf[8];
#pragma unroll
        for (int e = 0; e < 4; ++e) { wf[2 * e] = bf_lo(w[e]); wf[2 * e + 1] = bf_hi(w[e]); }
#pragma unroll
        for (int bb = 0; bb < 8; ++bb) {
          const float xv = xs[bb * DM + row];
#pragma unroll
          for (int e = 0; e < 8; ++e) a[bb][e] = fmaf(xv, wf[e], a[bb][e]);
        }
      }
#pragma unroll
      for (int bb = 0; bb < 8; ++bb)
#pragma unroll
        for (int e = 0; e < 8; ++e) {
          float v = a[bb][e];
          v += __shfl_xor(v, 8);
          v += __shfl_xor(v, 16);
          v += __shfl_xor(v, 32);
          a[bb][e] = v;
        }
      if (lane < 8) {
        float* dst = red + ((mat * 4 + (wave & 3)) * 8 + cc) * 64;
#pragma unroll
        for (int bb = 0; bb < 8; ++bb)
#pragma unroll
          for (int e = 0; e < 8; ++e) dst[bb * 8 + e] = a[bb][e];
      }
    }
    __syncthreads();
    {
      const int bb = t >> 6, u = t & 63;
      float g = 0.f, up = 0.f;
#pragma unroll
      for (int w = 0; w < 4; ++w) {
        g += red[((0 * 4 + w) * 8 + (u >> 3)) * 64 + bb * 8 + (u & 7)];
        up += red[((1 * 4 + w) * 8 + (u >> 3)) * 64 + bb * 8 + (u & 7)];
      }
      g = rbf(g);
      up = rbf(up);
      const float inner = 0.7978845608028654f * (g + 0.044715f * g * g * g);
      const float act = rbf(0.5f * g * (1.f + tanhf(inner)));
      hs[bb * FU + u] = rbf(act * up);
    }
    __syncthreads();
    for (int idx = t; idx < NB * 80; idx += NTHR) {
      const int bb = idx / 80, cc = idx % 80;
      float a[8];
#pragma unroll
      for (int e = 0; e < 8; ++e) a[e] = 0.f;
      const unsigned short* wrow = wd + (size_t)c * FU * DM + cc * 8;
#pragma unroll 8
      for (int u = 0; u < FU; ++u) {
        const u32x4 w = *reinterpret_cast<const u32x4*>(wrow + (size_t)u * DM);
        const float hv = hs[bb * FU + u];
#pragma unroll
        for (int e = 0; e < 4; ++e) {
          a[2 * e] = fmaf(hv, bf_lo(w[e]), a[2 * e]);
          a[2 * e + 1] = fmaf(hv, bf_hi(w[e]), a[2 * e + 1]);
        }
      }
      float* dst = ffn_part + ((size_t)c * NB + bb) * DM + cc * 8;
      *reinterpret_cast<f32x4*>(dst) = f32x4{a[0], a[1], a[2], a[3]};
      *reinterpret_cast<f32x4*>(dst + 4) = f32x4{a[4], a[5], a[6], a[7]};
    }
  }
}

__global__ void __launch_bounds__(64)
final_reduce(const unsigned short* __restrict__ x, const unsigned short* __restrict__ gw,
             const float* __restrict__ att_part, const float* __restrict__ ffn_part, unsigned short* __restrict__ out) {
  const int b = blockIdx.x / 10, cg = blockIdx.x % 10, t = threadIdx.x;
  const int n = cg * 64 + t;
  float z = 0.f;
  for (int i = t; i < DM; i += 64) {
    const float xv = __builtin_bit_cast(float, (u32)x[b * DM + i] << 16);
    const float gv = __builtin_bit_cast(float, (u32)gw[i] << 16);
    z += rbf(xv * gv);
  }
#pragma unroll
  for (int o = 32; o >= 1; o >>= 1) z += __shfl_xor(z, o);
  z = rbf(z);
  const float g = rbf(1.f / (1.f + __expf(-z)));
  float a = 0.f;
#pragma unroll 8
  for (int hh = 0; hh < HQ; ++hh) a += att_part[(size_t)(b * HQ + hh) * DM + n];
  float f = 0.f;
#pragma unroll 8
  for (int c = 0; c < NFFN; ++c) f += ffn_part[((size_t)c * NB + b) * DM + n];
  a = rbf(a);
  f = rbf(f);
  const float omg = rbf(1.f - g);
  const float r = rbf(g * a) + rbf(omg * f);
  out[b * DM + n] = f2bf_bits(r);
}

extern "C" void kernel_launch(void* const* d_in, const int* in_sizes, int n_in,
                              void* d_out, int out_size, void* d_ws, size_t ws_size,
                              hipStream_t stream) {
  const unsigned short* x = (const unsigned short*)d_in[0];
  const unsigned short* kc = (const unsigned short*)d_in[1];
  const unsigned short* vc = (const unsigned short*)d_in[2];
  const unsigned short* wq = (const unsigned short*)d_in[3];
  const unsigned short* wo = (const unsigned short*)d_in[4];
  const unsigned short* wg = (const unsigned short*)d_in[6];
  const unsigned short* wu = (const unsigned short*)d_in[7];
  const unsigned short* wd = (const unsigned short*)d_in[8];
  const unsigned short* gw = (const unsigned short*)d_in[9];
  float* att_part = (float*)((char*)d_ws + WS_ATT);
  float* ffn_part = (float*)((char*)d_ws + WS_FFN);
  hipLaunchKernelGGL(fused_main, dim3(NATT + NFFN), dim3(NTHR), 0, stream, x, kc, vc, wq, wo, wg, wu, wd, att_part, ffn_part);
  hipLaunchKernelGGL(final_reduce, dim3(NB * 10), dim3(64), 0, stream, x, gw, (const float*)att_part, (const float*)ffn_part, (unsigned short*)d_out);
}
```

```cpp
#include <hip/hip_runtime.h>
#include <hip/hip_bf16.h>
#include <stdint.h>

typedef unsigned int u32;
using u32x4 = __attribute__((ext_vector_type(4))) unsigned;
using f32x4 = __attribute__((ext_vector_type(4))) float;
using bf16x2 = __attribute__((ext_vector_type(2))) __bf16;

constexpr int NB = 8, DM = 640, HQ = 32, SK = 4096, DH = 192, WIN = 1024, FF = 1536;
constexpr int NATT = NB * HQ;
constexpr int NFFN = 48;
constexpr int FU = FF / NFFN;
constexpr int NTHR = 512;
constexpr int NWAVE = NTHR / 64;
constexpr int KEYS_PER_WAVE = WIN / NWAVE;
constexpr int NST = KEYS_PER_WAVE / 8;
#define ABL_NBUF 2
constexpr int NBUF = ABL_NBUF;

constexpr size_t WS_ATT = 0;
constexpr size_t WS_FFN = (size_t)NATT * DM * 4;
constexpr size_t WS_END = WS_FFN + (size_t)NFFN * NB * DM * 4;

__device__ __forceinline__ float bf_lo(u32 x) { return __builtin_bit_cast(float, x << 16); }
__device__ __forceinline__ float bf_hi(u32 x) { return __builtin_bit_cast(float, x & 0xffff0000u); }
__device__ __forceinline__ float rbf(float x) {
  u32 u = __builtin_bit_cast(u32, x);
  u = (u + 0x7fffu + ((u >> 16) & 1u)) & 0xffff0000u;
  return __builtin_bit_cast(float, u);
}
__device__ __forceinline__ unsigned short f2bf_bits(float x) {
  u32 u = __builtin_bit_cast(u32, x);
  u = (u + 0x7fffu + ((u >> 16) & 1u));
  return (unsigned short)(u >> 16);
}
__device__ __forceinline__ float dot2(u32 k, u32 q, float acc) {
  return __builtin_amdgcn_fdot2_f32_bf16(__builtin_bit_cast(bf16x2, k), __builtin_bit_cast(bf16x2, q), acc, false);
}
template <int CTRL> __device__ __forceinline__ float dppf(float x) {
  return __builtin_bit_cast(float, __builtin_amdgcn_mov_dpp(__builtin_bit_cast(int, x), CTRL, 0xf, 0xf, true));
}

#define WLOAD(p) (*(p))
constexpr int LDS_FLOATS = 64 * DH + 1024;

__global__ void __launch_bounds__(NTHR, 4)
fused_main(const unsigned short* __restrict__ x, const unsigned short* __restrict__ kc, const unsigned short* __restrict__ vc,
           const unsigned short* __restrict__ wq, const unsigned short* __restrict__ wo,
           const unsigned short* __restrict__ wg, const unsigned short* __restrict__ wu, const unsigned short* __restrict__ wd,
           float* __restrict__ att_part, float* __restrict__ ffn_part) {
  __shared__ __attribute__((aligned(16))) float lds[LDS_FLOATS];
  const int t = threadIdx.x;
  const int lane = t & 63, wave = t >> 6;

  if (blockIdx.x < NATT) {
    const int bid = blockIdx.x;
    const int xg = bid & 7, r = bid >> 3;
    const int h = (r & 3) * 8 + xg;
    const int b = r >> 2;
    const int rs = lane >> 3, sub = lane & 7;

    const size_t kvbase = ((size_t)(b * HQ + h) * SK + (SK - WIN) + wave * KEYS_PER_WAVE + rs) * DH + sub * 8;
    const u32x4* kp = reinterpret_cast<const u32x4*>(kc + kvbase);
    const u32x4* vp = reinterpret_cast<const u32x4*>(vc + kvbase);

    float* xs = lds;
    float* red = lds + 640;
    unsigned short* qs = reinterpret_cast<unsigned short*>(lds + 640 + 20 * DH);
    const int qcol = t % 24, qrg = t / 24;
    const bool qact = t < 480;
    const unsigned short* wrow = wq + (size_t)qrg * (HQ * DH) + h * DH + qcol * 8;
    constexpr int QG = 4;
    u32x4 w0[QG], w1[QG];
    if (qact) {
#pragma unroll
      for (int i = 0; i < QG; ++i) w0[i] = WLOAD(reinterpret_cast<const u32x4*>(wrow + (size_t)i * 20 * (HQ * DH)));
#pragma unroll
      for (int i = 0; i < QG; ++i) w1[i] = WLOAD(reinterpret_cast<const u32x4*>(wrow + (size_t)(QG + i) * 20 * (HQ * DH)));
    }
    if (t < DM / 8) {
      const u32x4 xv = *reinterpret_cast<const u32x4*>(x + b * DM + t * 8);
      f32x4 lo, hi;
#pragma unroll
      for (int e = 0; e < 2; ++e) { lo[2 * e] = bf_lo(xv[e]); lo[2 * e + 1] = bf_hi(xv[e]); hi[2 * e] = bf_lo(xv[2 + e]); hi[2 * e + 1] = bf_hi(xv[2 + e]); }
      *reinterpret_cast<f32x4*>(xs + t * 8) = lo;
      *reinterpret_cast<f32x4*>(xs + t * 8 + 4) = hi;
    }

    u32x4 kb[NBUF][3], vb[NBUF][3];
#pragma unroll
    for (int s = 0; s < 1; ++s) {
#pragma unroll
      for (int j = 0; j < 3; ++j) {
        kb[s][j] = __builtin_nontemporal_load(kp + s * 192 + j * 8);
        vb[s][j] = __builtin_nontemporal_load(vp + s * 192 + j * 8);
      }
    }
    __syncthreads();
    if (qact) {
      float a[8];
#pragma unroll
      for (int e = 0; e < 8; ++e) a[e] = 0.f;
      auto qfma = [&](const u32x4 (&w)[QG], int it0) {
#pragma unroll
        for (int i = 0; i < QG; ++i) {
          const float xv = xs[(it0 + i) * 20 + qrg];
#pragma unroll
          for (int e = 0; e < 4; ++e) {
            a[2 * e] = fmaf(xv, bf_lo(w[i][e]), a[2 * e]);
            a[2 * e + 1] = fmaf(xv, bf_hi(w[i][e]), a[2 * e + 1]);
          }
        }
      };
#pragma unroll
      for (int it0 = 0; it0 < 32; it0 += 2 * QG) {
        qfma(w0, it0);
        if (it0 + 2 * QG < 32) {
#pragma unroll
          for (int i = 0; i < QG; ++i) w0[i] = WLOAD(reinterpret_cast<const u32x4*>(wrow + (size_t)(it0 + 2 * QG + i) * 20 * (HQ * DH)));
        }
        qfma(w1, it0 + QG);
        if (it0 + 3 * QG < 32) {
#pragma unroll
          for (int i = 0; i < QG; ++i) w1[i] = WLOAD(reinterpret_cast<const u32x4*>(wrow + (size_t)(it0 + 3 * QG + i) * 20 * (HQ * DH)));
        }
      }
#pragma unroll
      for (int e = 0; e < 8; ++e) red[qrg * DH + qcol * 8 + e] = a[e];
    }
#pragma unroll
    for (int s = 1; s < NBUF; ++s) {
#pragma unroll
      for (int j = 0; j < 3; ++j) {
        kb[s][j] = __builtin_nontemporal_load(kp + s * 192 + j * 8);
        vb[s][j] = __builtin_nontemporal_load(vp + s * 192 + j * 8);
      }
    }
    __syncthreads();
    if (t < DH) {
      float s = 0.f;
#pragma unroll
      for (int g = 0; g < 20; ++g) s += red[g * DH + t];
      qs[t] = f2bf_bits(s);
    }
    __syncthreads();
    u32x4 qv[3];
#pragma unroll
    for (int j = 0; j < 3; ++j) qv[j] = *reinterpret_cast<const u32x4*>(qs + (sub + 8 * j) * 8);

    const float SC = 0.07216878364870322f * 1.4426950408889634f;
    float m = -INFINITY, l = 0.f;
    float acc[24];
#pragma unroll
    for (int i = 0; i < 24; ++i) acc[i] = 0.f;

    auto compute = [&](const u32x4 (&kk)[3], const u32x4 (&vv3)[3]) {
      float sc = 0.f;
#pragma unroll
      for (int j = 0; j < 3; ++j)
#pragma unroll
        for (int w = 0; w < 4; ++w) sc = dot2(kk[j][w], qv[j][w], sc);
      sc += dppf<0xB1>(sc);
      sc += dppf<0x4E>(sc);
      sc += dppf<0x141>(sc);
      sc *= SC;
      const float mn = fmaxf(m, sc);
      const float alpha = __builtin_amdgcn_exp2f(m - mn);
      const float p = __builtin_amdgcn_exp2f(sc - mn);
      l = fmaf(l, alpha, p);
      m = mn;
#pragma unroll
      for (int j = 0; j < 3; ++j)
#pragma unroll
        for (int w = 0; w < 4; ++w) {
          const u32 vv = vv3[j][w];
          acc[j * 8 + 2 * w] = fmaf(p, bf_lo(vv), acc[j * 8 + 2 * w] * alpha);
          acc[j * 8 + 2 * w + 1] = fmaf(p, bf_hi(vv), acc[j * 8 + 2 * w + 1] * alpha);
        }
    };
#pragma unroll 1
    for (int s0 = 0; s0 < NST - NBUF; s0 += NBUF) {
#pragma unroll
      for (int cur = 0; cur < NBUF; ++cur) {
        compute(kb[cur], vb[cur]);
#pragma unroll
        for (int j = 0; j < 3; ++j) {
          kb[cur][j] = __builtin_nontemporal_load(kp + (s0 + cur + NBUF) * 192 + j * 8);
          vb[cur][j] = __builtin_nontemporal_load(vp + (s0 + cur + NBUF) * 192 + j * 8);
        }
      }
    }
#pragma unroll
    for (int cur = 0; cur < NBUF; ++cur) compute(kb[cur], vb[cur]);

    float* mlm = lds + 64 * DH;
    float* mll = mlm + 64;
    float* buf = lds;
    const int slot = wave * 8 + rs;
    __syncthreads();
    if (sub == 0) { mlm[slot] = m; mll[slot] = l; }
    __syncthreads();
    float M = -INFINITY;
#pragma unroll 8
    for (int i = 0; i < 64; ++i) M = fmaxf(M, mlm[i]);
    float L = 0.f;
#pragma unroll 8
    for (int i = 0; i < 64; ++i) L += mll[i] * __builtin_amdgcn_exp2f(mlm[i] - M);
    const float msc = __builtin_amdgcn_exp2f(m - M);
#pragma unroll
    for (int j = 0; j < 3; ++j) {
      f32x4 o0, o1;
#pragma unroll
      for (int e = 0; e < 4; ++e) { o0[e] = acc[j * 8 + e] * msc; o1[e] = acc[j * 8 + 4 + e] * msc; }
      float* dst = buf + slot * DH + (sub + 8 * j) * 8;
      *reinterpret_cast<f32x4*>(dst) = o0;
      *reinterpret_cast<f32x4*>(dst + 4) = o1;
    }
    __syncthreads();
    float ov = 0.f;
    if (t < DH) {
#pragma unroll 8
      for (int i = 0; i < 64; ++i) ov += buf[i * DH + t];
      ov = rbf(ov / L);
    }
    __syncthreads();
    float* os = lds;
    float* red2 = lds + 256;
    if (t < DH) os[t] = ov;
    __syncthreads();

    if (t < 480) {
      const int cc = t % 80, rg = t / 80;
      float a[8];
#pragma unroll
      for (int e = 0; e < 8; ++e) a[e] = 0.f;
      const unsigned short* wrow2 = wo + (size_t)(h * DH + rg * 32) * DM + cc * 8;
#pragma unroll 8
      for (int i = 0; i < 32; ++i) {
        const u32x4 w = WLOAD(reinterpret_cast<const u32x4*>(wrow2 + (size_t)i * DM));
        const float o = os[rg * 32 + i];
#pragma unroll
        for (int e = 0; e < 4; ++e) {
          a[2 * e] = fmaf(o, bf_lo(w[e]), a[2 * e]);
          a[2 * e + 1] = fmaf(o, bf_hi(w[e]), a[2 * e + 1]);
        }
      }
#pragma unroll
      for (int e = 0; e < 8; ++e) red2[rg * DM + cc * 8 + e] = a[e];
    }
    __syncthreads();
    for (int n = t; n < DM; n += NTHR) {
      float s = 0.f;
#pragma unroll
      for (int g = 0; g < 6; ++g) s += red2[g * DM + n];
      att_part[(size_t)(b * HQ + h) * DM + n] = s;
    }
  } else {
    const int c = blockIdx.x - NATT;
    __builtin_amdgcn_s_setprio(3);
    float* xs = lds;
    float* red = lds + NB * DM;
    float* hs = red + 2 * 4 * 8 * 32;
    const int mat = wave >> 2, rgw = wave & 3;
    const int cc = lane & 3, rgl = lane >> 2;
    const int rg = rgw * 16 + rgl;
    const unsigned short* W = (mat ? wu : wg) + (size_t)c * FU + cc * 8;
    u32x4 w1r[10];
#pragma unroll
    for (int i = 0; i < 10; ++i) w1r[i] = WLOAD(reinterpret_cast<const u32x4*>(W + (size_t)(rg + 64 * i) * FF));
    const int ccl = lane & 15, ug = lane >> 4;
    const int cc2 = wave * 16 + ccl;
    u32x4 w2r[8];
    if (wave < 5) {
#pragma unroll
      for (int i = 0; i < 8; ++i) w2r[i] = WLOAD(reinterpret_cast<const u32x4*>(wd + (size_t)(c * FU + ug * 8 + i) * DM + cc2 * 8));
    }
    {
      u32x4 xv[2];
#pragma unroll
      for (int k = 0; k < 2; ++k) { const int ch = min(t + k * NTHR, NB * DM / 8 - 1); xv[k] = *reinterpret_cast<const u32x4*>(x + ch * 8); }
#pragma unroll
      for (int k = 0; k < 2; ++k) {
        const int ch = t + k * NTHR;
        if (ch < NB * DM / 8) {
          f32x4 lo, hi;
#pragma unroll
          for (int e = 0; e < 2; ++e) { lo[2 * e] = bf_lo(xv[k][e]); lo[2 * e + 1] = bf_hi(xv[k][e]); hi[2 * e] = bf_lo(xv[k][2 + e]); hi[2 * e + 1] = bf_hi(xv[k][2 + e]); }
          *reinterpret_cast<f32x4*>(xs + ch * 8) = lo;
          *reinterpret_cast<f32x4*>(xs + ch * 8 + 4) = hi;
        }
      }
    }
    __syncthreads();
#pragma unroll 1
    for (int bb = 0; bb < NB; ++bb) {
      float a[8];
#pragma unroll
      for (int e = 0; e < 8; ++e) a[e] = 0.f;
#pragma unroll
      for (int i = 0; i < 10; ++i) {
        asm volatile("" : "+v"(w1r[i]));
        const float xv = xs[bb * DM + rg + 64 * i];
#pragma unroll
        for (int e = 0; e < 4; ++e) {
          a[2 * e] = fmaf(xv, bf_lo(w1r[i][e]), a[2 * e]);
          a[2 * e + 1] = fmaf(xv, bf_hi(w1r[i][e]), a[2 * e + 1]);
        }
      }
#pragma unroll
      for (int e = 0; e < 8; ++e) {
        float v = a[e];
        v += __shfl_xor(v, 4);
        v += __shfl_xor(v, 8);
        v += __shfl_xor(v, 16);
        v += __shfl_xor(v, 32);
        a[e] = v;
      }
      if (lane < 4) {
#pragma unroll
        for (int e = 0; e < 8; ++e) red[((mat * 4 + rgw) * 8 + bb) * 32 + cc * 8 + e] = a[e];
      }
    }
    __syncthreads();
    if (t < NB * FU) {
      const int bb = t >> 5, u = t & 31;
      float g = 0.f, up = 0.f;
#pragma unroll
      for (int w = 0; w < 4; ++w) {
        g += red[((0 * 4 + w) * 8 + bb) * 32 + u];
        up += red[((1 * 4 + w) * 8 + bb) * 32 + u];
      }
      g = rbf(g);
      up = rbf(up);
      const float inner = 0.7978845608028654f * (g + 0.044715f * g * g * g);
      const float act = rbf(0.5f * g * (1.f + tanhf(inner)));
      hs[bb * FU + u] = rbf(act * up);
    }
    __syncthreads();
    if (wave < 5) {
#pragma unroll 1
      for (int bb = 0; bb < NB; ++bb) {
        float a[8];
#pragma unroll
        for (int e = 0; e < 8; ++e) a[e] = 0.f;
#pragma unroll
        for (int i = 0; i < 8; ++i) {
          asm volatile("" : "+v"(w2r[i]));
          const float hv = hs[bb * FU + ug * 8 + i];
#pragma unroll
          for (int e = 0; e < 4; ++e) {
            a[2 * e] = fmaf(hv, bf_lo(w2r[i][e]), a[2 * e]);
            a[2 * e + 1] = fmaf(hv, bf_hi(w2r[i][e]), a[2 * e + 1]);
          }
        }
#pragma unroll
        for (int e = 0; e < 8; ++e) {
          float v = a[e];
          v += __shfl_xor(v, 16);
          v += __shfl_xor(v, 32);
          a[e] = v;
        }
        if (lane < 16) {
          float* dst = ffn_part + ((size_t)c * NB + bb) * DM + cc2 * 8;
          *reinterpret_cast<f32x4*>(dst) = f32x4{a[0], a[1], a[2], a[3]};
          *reinterpret_cast<f32x4*>(dst + 4) = f32x4{a[4], a[5], a[6], a[7]};
        }
      }
    }
  }
}

__global__ void __launch_bounds__(256)
final_reduce(const unsigned short* __restrict__ x, const unsigned short* __restrict__ gw,
             const float* __restrict__ att_part, const float* __restrict__ ffn_part, unsigned short* __restrict__ out) {
  __shared__ float sa[4][64], sf[4][64], sz[4];
  const int b = blockIdx.x / 10, cg = blockIdx.x % 10, t = threadIdx.x;
  const int nl = t & 63, pg = t >> 6;
  const int n = cg * 64 + nl;
  float a = 0.f, f = 0.f;
  float av[8], fv[12];
#pragma unroll
  for (int i = 0; i < 8; ++i) av[i] = att_part[(size_t)(b * HQ + pg * 8 + i) * DM + n];
#pragma unroll
  for (int i = 0; i < 12; ++i) fv[i] = ffn_part[((size_t)(pg * 12 + i) * NB + b) * DM + n];
  float z = 0.f;
  for (int i = t; i < DM; i += 256) {
    const float xv = __builtin_bit_cast(float, (u32)x[b * DM + i] << 16);
    const float gv = __builtin_bit_cast(float, (u32)gw[i] << 16);
    z += rbf(xv * gv);
  }
#pragma unroll
  for (int i = 0; i < 8; ++i) a += av[i];
#pragma unroll
  for (int i = 0; i < 12; ++i) f += fv[i];
#pragma unroll
  for (int o = 32; o >= 1; o >>= 1) z += __shfl_xor(z, o);
  sa[pg][nl] = a; sf[pg][nl] = f;
  if (nl == 0) sz[pg] = z;
  __syncthreads();
  if (t < 64) {
    z = rbf(sz[0] + sz[1] + sz[2] + sz[3]);
    const float g = rbf(1.f / (1.f + __expf(-z)));
    a = rbf((sa[0][nl] + sa[1][nl]) + (sa[2][nl] + sa[3][nl]));
    f = rbf((sf[0][nl] + sf[1][nl]) + (sf[2][nl] + sf[3][nl]));
    const float omg = rbf(1.f - g);
    const float r = rbf(g * a) + rbf(omg * f);
    out[b * DM + n] = f2bf_bits(r);
  }
}

extern "C" void kernel_launch(void* const* d_in, const int* in_sizes, int n_in,
                              void* d_out, int out_size, void* d_ws, size_t ws_size,
                              hipStream_t stream) {
  const unsigned short* x = (const unsigned short*)d_in[0];
  const unsigned short* kc = (const unsigned short*)d_in[1];
  const unsigned short* vc = (const unsigned short*)d_in[2];
  const unsigned short* wq = (const unsigned short*)d_in[3];
  const unsigned short* wo = (const unsigned short*)d_in[4];
  const unsigned short* wg = (const unsigned short*)d_in[6];
  const unsigned short* wu = (const unsigned short*)d_in[7];
  const unsigned short* wd = (const unsigned short*)d_in[8];
  const unsigned short* gw = (const unsigned short*)d_in[9];
  float* att_part = (float*)((char*)d_ws + WS_ATT);
  float* ffn_part = (float*)((char*)d_ws + WS_FFN);
  constexpr int GR = NATT + NFFN;
  hipLaunchKernelGGL(fused_main, dim3(GR), dim3(NTHR), 0, stream, x, kc, vc, wq, wo, wg, wu, wd, att_part, ffn_part);
  hipLaunchKernelGGL(final_reduce, dim3(NB * 10), dim3(256), 0, stream, x, gw, (const float*)att_part, (const float*)ffn_part, (unsigned short*)d_out);
}
```

```cpp
#include <hip/hip_runtime.h>
#include <hip/hip_bf16.h>
#include <stdint.h>

typedef unsigned int u32;
using u32x4 = __attribute__((ext_vector_type(4))) unsigned;
using f32x4 = __attribute__((ext_vector_type(4))) float;
using bf16x2 = __attribute__((ext_vector_type(2))) __bf16;

constexpr int NB = 8, DM = 640, HQ = 32, SK = 4096, DH = 192, WIN = 1024, FF = 1536;
constexpr int NATT = NB * HQ;
constexpr int UPH = FF / HQ;
constexpr int NTHR = 512;
constexpr int NWAVE = NTHR / 64;
constexpr int KEYS_PER_WAVE = WIN / NWAVE;
constexpr int NST = KEYS_PER_WAVE / 8;
#define ABL_NBUF 4
constexpr int NBUF = ABL_NBUF;

__device__ __forceinline__ float bf_lo(u32 x) { return __builtin_bit_cast(float, x << 16); }
__device__ __forceinline__ float bf_hi(u32 x) { return __builtin_bit_cast(float, x & 0xffff0000u); }
__device__ __forceinline__ float rbf(float x) {
  u32 u = __builtin_bit_cast(u32, x);
  u = (u + 0x7fffu + ((u >> 16) & 1u)) & 0xffff0000u;
  return __builtin_bit_cast(float, u);
}
__device__ __forceinline__ unsigned short f2bf_bits(float x) {
  u32 u = __builtin_bit_cast(u32, x);
  u = (u + 0x7fffu + ((u >> 16) & 1u));
  return (unsigned short)(u >> 16);
}
__device__ __forceinline__ float dot2(u32 k, u32 q, float acc) {
  return __builtin_amdgcn_fdot2_f32_bf16(__builtin_bit_cast(bf16x2, k), __builtin_bit_cast(bf16x2, q), acc, false);
}
template <int CTRL> __device__ __forceinline__ float dppf(float x) {
  return __builtin_bit_cast(float, __builtin_amdgcn_mov_dpp(__builtin_bit_cast(int, x), CTRL, 0xf, 0xf, true));
}
__device__ __forceinline__ void fma8(float (&a)[8], float s, const u32x4& w) {
#pragma unroll
  for (int e = 0; e < 4; ++e) {
    a[2 * e] = fmaf(s, bf_lo(w[e]), a[2 * e]);
    a[2 * e + 1] = fmaf(s, bf_hi(w[e]), a[2 * e + 1]);
  }
}

constexpr int L_XS = 0, L_REDA = 640, L_REDB = L_REDA + 20 * DH, L_QS = 64 * DH, L_HID = L_QS + 96, L_Z = L_HID + 64, L_ML = L_Z + 96, L_OS = L_ML + 128, L_RED2 = L_OS + 256;
constexpr int LDS_FLOATS = L_RED2 + 6 * DM;

__global__ void __launch_bounds__(NTHR, 2)
fused_main(const unsigned short* __restrict__ x, const unsigned short* __restrict__ kc, const unsigned short* __restrict__ vc,
           const unsigned short* __restrict__ wq, const unsigned short* __restrict__ wo,
           const unsigned short* __restrict__ wg, const unsigned short* __restrict__ wu, const unsigned short* __restrict__ wd,
           const unsigned short* __restrict__ gw, float* __restrict__ part) {
  __shared__ __attribute__((aligned(16))) float lds[LDS_FLOATS];
  const int t = threadIdx.x;
  const int lane = t & 63, wave = t >> 6;
  const int bid = blockIdx.x;
  const int xg = bid & 7, r = bid >> 3;
  const int h = (r & 3) * 8 + xg;
  const int b = r >> 2;
  const int rs = lane >> 3, sub = lane & 7;

  float* xs = lds + L_XS;
  float* redA = lds + L_REDA;
  float* redB = lds + L_REDB;
  unsigned short* qs = reinterpret_cast<unsigned short*>(lds + L_QS);
  float* hid = lds + L_HID;
  float* zred = lds + L_Z;

  const size_t kvbase = ((size_t)(b * HQ + h) * SK + (SK - WIN) + wave * KEYS_PER_WAVE + rs) * DH + sub * 8;
  const u32x4* kp = reinterpret_cast<const u32x4*>(kc + kvbase);
  const u32x4* vp = reinterpret_cast<const u32x4*>(vc + kvbase);

  const bool pact = t < 480;
  const int qcol = t % 24, qrg = t / 24;
  const unsigned short* wrowA = wq + (size_t)qrg * (HQ * DH) + h * DH + qcol * 8;
  const int c12 = t % 12, frg = t / 12;
  const unsigned short* wrowB = (c12 < 6 ? wg : wu) + (size_t)frg * FF + h * UPH + (c12 % 6) * 8;
  u32x4 w0[8], w1[8];
  if (pact) {
#pragma unroll
    for (int i = 0; i < 8; ++i) w0[i] = __builtin_nontemporal_load(reinterpret_cast<const u32x4*>(wrowA + (size_t)i * 20 * (HQ * DH)));
#pragma unroll
    for (int i = 0; i < 8; ++i) w1[i] = __builtin_nontemporal_load(reinterpret_cast<const u32x4*>(wrowA + (size_t)(8 + i) * 20 * (HQ * DH)));
  }
  if (t < DM / 8) {
    const u32x4 xv = *reinterpret_cast<const u32x4*>(x + b * DM + t * 8);
    const u32x4 gv = *reinterpret_cast<const u32x4*>(gw + t * 8);
    f32x4 lo, hi;
    float z = 0.f;
#pragma unroll
    for (int e = 0; e < 2; ++e) {
      lo[2 * e] = bf_lo(xv[e]); lo[2 * e + 1] = bf_hi(xv[e]); hi[2 * e] = bf_lo(xv[2 + e]); hi[2 * e + 1] = bf_hi(xv[2 + e]);
    }
#pragma unroll
    for (int e = 0; e < 4; ++e) { z += rbf(bf_lo(xv[e]) * bf_lo(gv[e])); z += rbf(bf_hi(xv[e]) * bf_hi(gv[e])); }
    *reinterpret_cast<f32x4*>(xs + t * 8) = lo;
    *reinterpret_cast<f32x4*>(xs + t * 8 + 4) = hi;
    zred[t] = z;
  }
  u32x4 kb[NBUF][3], vb[NBUF][3];
#pragma unroll
  for (int s = 0; s < NBUF; ++s)
#pragma unroll
    for (int j = 0; j < 3; ++j) {
      kb[s][j] = __builtin_nontemporal_load(kp + s * 192 + j * 8);
      vb[s][j] = __builtin_nontemporal_load(vp + s * 192 + j * 8);
    }
  __syncthreads();
  if (pact) {
    float a[8];
#pragma unroll
    for (int e = 0; e < 8; ++e) a[e] = 0.f;
#pragma unroll
    for (int i = 0; i < 8; ++i) fma8(a, xs[i * 20 + qrg], w0[i]);
#pragma unroll
    for (int i = 0; i < 8; ++i) w0[i] = __builtin_nontemporal_load(reinterpret_cast<const u32x4*>(wrowA + (size_t)(16 + i) * 20 * (HQ * DH)));
#pragma unroll
    for (int i = 0; i < 8; ++i) fma8(a, xs[(8 + i) * 20 + qrg], w1[i]);
#pragma unroll
    for (int i = 0; i < 8; ++i) w1[i] = __builtin_nontemporal_load(reinterpret_cast<const u32x4*>(wrowA + (size_t)(24 + i) * 20 * (HQ * DH)));
#pragma unroll
    for (int i = 0; i < 8; ++i) fma8(a, xs[(16 + i) * 20 + qrg], w0[i]);
#pragma unroll
    for (int i = 0; i < 8; ++i) w0[i] = __builtin_nontemporal_load(reinterpret_cast<const u32x4*>(wrowB + (size_t)i * 40 * FF));
#pragma unroll
    for (int i = 0; i < 8; ++i) fma8(a, xs[(24 + i) * 20 + qrg], w1[i]);
#pragma unroll
    for (int i = 0; i < 8; ++i) w1[i] = __builtin_nontemporal_load(reinterpret_cast<const u32x4*>(wrowB + (size_t)(8 + i) * 40 * FF));
#pragma unroll
    for (int e = 0; e < 8; ++e) { redA[qrg * DH + qcol * 8 + e] = a[e]; a[e] = 0.f; }
#pragma unroll
    for (int i = 0; i < 8; ++i) fma8(a, xs[i * 40 + frg], w0[i]);
#pragma unroll
    for (int i = 0; i < 8; ++i) fma8(a, xs[(8 + i) * 40 + frg], w1[i]);
#pragma unroll
    for (int e = 0; e < 8; ++e) redB[frg * 96 + c12 * 8 + e] = a[e];
  }
  __syncthreads();
  if (t < DH) {
    float s = 0.f;
#pragma unroll
    for (int g = 0; g < 20; ++g) s += redA[g * DH + t];
    qs[t] = f2bf_bits(s);
  } else if (t < DH + UPH) {
    const int u = t - DH;
    float g = 0.f, up = 0.f;
#pragma unroll
    for (int k = 0; k < 40; ++k) { g += redB[k * 96 + u]; up += redB[k * 96 + UPH + u]; }
    g = rbf(g);
    up = rbf(up);
    const float inner = 0.7978845608028654f * (g + 0.044715f * g * g * g);
    const float act = rbf(0.5f * g * (1.f + tanhf(inner)));
    hid[u] = rbf(act * up);
  }
  __syncthreads();
  u32x4 qv[3];
#pragma unroll
  for (int j = 0; j < 3; ++j) qv[j] = *reinterpret_cast<const u32x4*>(qs + (sub + 8 * j) * 8);

  const float SC = 0.07216878364870322f * 1.4426950408889634f;
  float m = -INFINITY, l = 0.f;
  float acc[24];
#pragma unroll
  for (int i = 0; i < 24; ++i) acc[i] = 0.f;
  auto compute = [&](const u32x4 (&kk)[3], const u32x4 (&vv3)[3]) {
    float sc = 0.f;
#pragma unroll
    for (int j = 0; j < 3; ++j)
#pragma unroll
      for (int w = 0; w < 4; ++w) sc = dot2(kk[j][w], qv[j][w], sc);
    sc += dppf<0xB1>(sc);
    sc += dppf<0x4E>(sc);
    sc += dppf<0x141>(sc);
    sc *= SC;
    const float mn = fmaxf(m, sc);
    const float alpha = __builtin_amdgcn_exp2f(m - mn);
    const float p = __builtin_amdgcn_exp2f(sc - mn);
    l = fmaf(l, alpha, p);
    m = mn;
#pragma unroll
    for (int j = 0; j < 3; ++j)
#pragma unroll
      for (int w = 0; w < 4; ++w) {
        const u32 vv = vv3[j][w];
        acc[j * 8 + 2 * w] = fmaf(p, bf_lo(vv), acc[j * 8 + 2 * w] * alpha);
        acc[j * 8 + 2 * w + 1] = fmaf(p, bf_hi(vv), acc[j * 8 + 2 * w + 1] * alpha);
      }
  };
#pragma unroll 1
  for (int s0 = 0; s0 < NST - NBUF; s0 += NBUF) {
#pragma unroll
    for (int cur = 0; cur < NBUF; ++cur) {
      compute(kb[cur], vb[cur]);
#pragma unroll
      for (int j = 0; j < 3; ++j) {
        kb[cur][j] = __builtin_nontemporal_load(kp + (s0 + cur + NBUF) * 192 + j * 8);
        vb[cur][j] = __builtin_nontemporal_load(vp + (s0 + cur + NBUF) * 192 + j * 8);
      }
    }
  }
#pragma unroll
  for (int cur = 0; cur < NBUF; ++cur) compute(kb[cur], vb[cur]);

#pragma unroll
  for (int i = 0; i < 24; ++i) asm volatile("" : "+v"(acc[i]));
  asm volatile("" ::: "memory");
  const int cc = t % 80, rg = t / 80;
  u32x4 wo_r[32], wd_r[8];
  if (pact) {
    const unsigned short* wrow2 = wo + (size_t)(h * DH + rg * 32) * DM + cc * 8;
#pragma unroll
    for (int i = 0; i < 32; ++i) wo_r[i] = __builtin_nontemporal_load(reinterpret_cast<const u32x4*>(wrow2 + (size_t)i * DM));
    const unsigned short* wrow3 = wd + (size_t)(h * UPH + rg * 8) * DM + cc * 8;
#pragma unroll
    for (int i = 0; i < 8; ++i) wd_r[i] = __builtin_nontemporal_load(reinterpret_cast<const u32x4*>(wrow3 + (size_t)i * DM));
  }

  float* mlm = lds + L_ML;
  float* mll = mlm + 64;
  float* buf = lds;
  float* os = lds + L_OS;
  float* red2 = lds + L_RED2;
  const int slot = wave * 8 + rs;
  if (sub == 0) { mlm[slot] = m; mll[slot] = l; }
  __syncthreads();
  float M = -INFINITY;
#pragma unroll 16
  for (int i = 0; i < 64; ++i) M = fmaxf(M, mlm[i]);
  float L = 0.f;
#pragma unroll 16
  for (int i = 0; i < 64; ++i) L += mll[i] * __builtin_amdgcn_exp2f(mlm[i] - M);
  const float msc = __builtin_amdgcn_exp2f(m - M);
#pragma unroll
  for (int j = 0; j < 3; ++j) {
    f32x4 o0, o1;
#pragma unroll
    for (int e = 0; e < 4; ++e) { o0[e] = acc[j * 8 + e] * msc; o1[e] = acc[j * 8 + 4 + e] * msc; }
    float* dst = buf + slot * DH + (sub + 8 * j) * 8;
    *reinterpret_cast<f32x4*>(dst) = o0;
    *reinterpret_cast<f32x4*>(dst + 4) = o1;
  }
  __syncthreads();
  if (t < DH) {
    float ov = 0.f;
#pragma unroll 16
    for (int i = 0; i < 64; ++i) ov += buf[i * DH + t];
    os[t] = rbf(ov / L);
  }
  __syncthreads();

  if (pact) {
    float z = 0.f;
#pragma unroll 16
    for (int i = 0; i < DM / 8; ++i) z += zred[i];
    z = rbf(z);
    const float g = rbf(1.f / (1.f + __expf(-z)));
    const float omg = rbf(1.f - g);
    float a[8], f[8];
#pragma unroll
    for (int e = 0; e < 8; ++e) { a[e] = 0.f; f[e] = 0.f; }
#pragma unroll
    for (int i = 0; i < 32; ++i) fma8(a, os[rg * 32 + i], wo_r[i]);
#pragma unroll
    for (int i = 0; i < 8; ++i) fma8(f, hid[rg * 8 + i], wd_r[i]);
#pragma unroll
    for (int e = 0; e < 8; ++e) red2[rg * DM + cc * 8 + e] = g * a[e] + omg * f[e];
  }
  __syncthreads();
  for (int n = t; n < DM; n += NTHR) {
    float s = 0.f;
#pragma unroll
    for (int g = 0; g < 6; ++g) s += red2[g * DM + n];
    part[(size_t)(b * HQ + h) * DM + n] = s;
  }
}

__global__ void __launch_bounds__(256)
final_reduce(const float* __restrict__ part, unsigned short* __restrict__ out) {
  __shared__ float sa[4][64];
  const int b = blockIdx.x / 10, cg = blockIdx.x % 10, t = threadIdx.x;
  const int nl = t & 63, pg = t >> 6;
  const int n = cg * 64 + nl;
  float av[8];
#pragma unroll
  for (int i = 0; i < 8; ++i) av[i] = part[(size_t)(b * HQ + pg * 8 + i) * DM + n];
  float a = 0.f;
#pragma unroll
  for (int i = 0; i < 8; ++i) a += av[i];
  sa[pg][nl] = a;
  __syncthreads();
  if (t < 64) out[b * DM + n] = f2bf_bits((sa[0][nl] + sa[1][nl]) + (sa[2][nl] + sa[3][nl]));
}

extern "C" void kernel_launch(void* const* d_in, const int* in_sizes, int n_in,
                              void* d_out, int out_size, void* d_ws, size_t ws_size,
                              hipStream_t stream) {
  const unsigned short* x = (const unsigned short*)d_in[0];
  const unsigned short* kc = (const unsigned short*)d_in[1];
  const unsigned short* vc = (const unsigned short*)d_in[2];
  const unsigned short* wq = (const unsigned short*)d_in[3];
  const unsigned short* wo = (const unsigned short*)d_in[4];
  const unsigned short* wg = (const unsigned short*)d_in[6];
  const unsigned short* wu = (const unsigned short*)d_in[7];
  const unsigned short* wd = (const unsigned short*)d_in[8];
  const unsigned short* gw = (const unsigned short*)d_in[9];
  float* part = (float*)d_ws;
  hipLaunchKernelGGL(fused_main, dim3(NATT), dim3(NTHR), 0, stream, x, kc, vc, wq, wo, wg, wu, wd, gw, part);
  hipLaunchKernelGGL(final_reduce, dim3(NB * 10), dim3(256), 0, stream, (const float*)part, (unsigned short*)d_out);
}
```

```cpp
#include <hip/hip_runtime.h>
#include <hip/hip_bf16.h>
#include <stdint.h>

typedef unsigned int u32;
using u32x4 = __attribute__((ext_vector_type(4))) unsigned;
using f32x4 = __attribute__((ext_vector_type(4))) float;
using bf16x2 = __attribute__((ext_vector_type(2))) __bf16;

constexpr int NB = 8, DM = 640, HQ = 32, SK = 4096, DH = 192, WIN = 1024, FF = 1536;
constexpr int NATT = NB * HQ;
constexpr int NFFN = 48;
constexpr int FU = FF / NFFN;
constexpr int NTHR = 512;
constexpr int NWAVE = NTHR / 64;
constexpr int KEYS_PER_WAVE = WIN / NWAVE;
constexpr int NST = KEYS_PER_WAVE / 8;
#define ABL_NBUF 2
constexpr int NBUF = ABL_NBUF;

constexpr size_t WS_ATT = 0;
constexpr size_t WS_FFN = (size_t)NATT * DM * 4;
constexpr size_t WS_END = WS_FFN + (size_t)NFFN * NB * DM * 4;

__device__ __forceinline__ float bf_lo(u32 x) { return __builtin_bit_cast(float, x << 16); }
__device__ __forceinline__ float bf_hi(u32 x) { return __builtin_bit_cast(float, x & 0xffff0000u); }
__device__ __forceinline__ float rbf(float x) {
  u32 u = __builtin_bit_cast(u32, x);
  u = (u + 0x7fffu + ((u >> 16) & 1u)) & 0xffff0000u;
  return __builtin_bit_cast(float, u);
}
__device__ __forceinline__ unsigned short f2bf_bits(float x) {
  u32 u = __builtin_bit_cast(u32, x);
  u = (u + 0x7fffu + ((u >> 16) & 1u));
  return (unsigned short)(u >> 16);
}
__device__ __forceinline__ float dot2(u32 k, u32 q, float acc) {
  return __builtin_amdgcn_fdot2_f32_bf16(__builtin_bit_cast(bf16x2, k), __builtin_bit_cast(bf16x2, q), acc, false);
}
template <int CTRL> __device__ __forceinline__ float dppf(float x) {
  return __builtin_bit_cast(float, __builtin_amdgcn_mov_dpp(__builtin_bit_cast(int, x), CTRL, 0xf, 0xf, true));
}

#define WLOAD(p) (*(p))
constexpr int LDS_FLOATS = 64 * DH + 1024;

__global__ void __launch_bounds__(NTHR, 4)
fused_main(const unsigned short* __restrict__ x, const unsigned short* __restrict__ kc, const unsigned short* __restrict__ vc,
           const unsigned short* __restrict__ wq, const unsigned short* __restrict__ wo,
           const unsigned short* __restrict__ wg, const unsigned short* __restrict__ wu, const unsigned short* __restrict__ wd,
           float* __restrict__ att_part, float* __restrict__ ffn_part) {
  __shared__ __attribute__((aligned(16))) float lds[LDS_FLOATS];
  const int t = threadIdx.x;
  const int lane = t & 63, wave = t >> 6;

  if (blockIdx.x < NATT) {
    const int bid = blockIdx.x;
    const int xg = bid & 7, r = bid >> 3;
    const int h = (r & 3) * 8 + xg;
    const int b = r >> 2;
    const int rs = lane >> 3, sub = lane & 7;

    const size_t kvbase = ((size_t)(b * HQ + h) * SK + (SK - WIN) + wave * KEYS_PER_WAVE + rs) * DH + sub * 8;
    const u32x4* kp = reinterpret_cast<const u32x4*>(kc + kvbase);
    const u32x4* vp = reinterpret_cast<const u32x4*>(vc + kvbase);

    float* xs = lds;
    float* red = lds + 640;
    unsigned short* qs = reinterpret_cast<unsigned short*>(lds + 640 + 20 * DH);
    const int qcol = t % 24, qrg = t / 24;
    const bool qact = t < 480;
    const unsigned short* wrow = wq + (size_t)qrg * (HQ * DH) + h * DH + qcol * 8;
#define ABL_QG 8
    constexpr int QG = ABL_QG;
    u32x4 w0[QG], w1[QG];
    if (qact) {
#pragma unroll
      for (int i = 0; i < QG; ++i) w0[i] = WLOAD(reinterpret_cast<const u32x4*>(wrow + (size_t)i * 20 * (HQ * DH)));
#pragma unroll
      for (int i = 0; i < QG; ++i) w1[i] = WLOAD(reinterpret_cast<const u32x4*>(wrow + (size_t)(QG + i) * 20 * (HQ * DH)));
    }
    if (t < DM / 8) {
      const u32x4 xv = *reinterpret_cast<const u32x4*>(x + b * DM + t * 8);
      f32x4 lo, hi;
#pragma unroll
      for (int e = 0; e < 2; ++e) { lo[2 * e] = bf_lo(xv[e]); lo[2 * e + 1] = bf_hi(xv[e]); hi[2 * e] = bf_lo(xv[2 + e]); hi[2 * e + 1] = bf_hi(xv[2 + e]); }
      *reinterpret_cast<f32x4*>(xs + t * 8) = lo;
      *reinterpret_cast<f32x4*>(xs + t * 8 + 4) = hi;
    }

    u32x4 kb[NBUF][3], vb[NBUF][3];
#pragma unroll
    for (int j = 0; j < 3; ++j) kb[0][j] = __builtin_nontemporal_load(kp + j * 8);
    __syncthreads();
    if (qact) {
      float a[8];
#pragma unroll
      for (int e = 0; e < 8; ++e) a[e] = 0.f;
      auto qfma = [&](const u32x4 (&w)[QG], int it0) {
#pragma unroll
        for (int i = 0; i < QG; ++i) {
          const float xv = xs[(it0 + i) * 20 + qrg];
#pragma unroll
          for (int e = 0; e < 4; ++e) {
            a[2 * e] = fmaf(xv, bf_lo(w[i][e]), a[2 * e]);
            a[2 * e + 1] = fmaf(xv, bf_hi(w[i][e]), a[2 * e + 1]);
          }
        }
      };
#pragma unroll
      for (int it0 = 0; it0 < 32; it0 += 2 * QG) {
        qfma(w0, it0);
        if (it0 + 2 * QG < 32) {
#pragma unroll
          for (int i = 0; i < QG; ++i) w0[i] = WLOAD(reinterpret_cast<const u32x4*>(wrow + (size_t)(it0 + 2 * QG + i) * 20 * (HQ * DH)));
        }
        qfma(w1, it0 + QG);
        if (it0 + 3 * QG < 32) {
#pragma unroll
          for (int i = 0; i < QG; ++i) w1[i] = WLOAD(reinterpret_cast<const u32x4*>(wrow + (size_t)(it0 + 3 * QG + i) * 20 * (HQ * DH)));
        }
      }
#pragma unroll
      for (int e = 0; e < 8; ++e) red[qrg * DH + qcol * 8 + e] = a[e];
    }
#pragma unroll
    for (int j = 0; j < 3; ++j) vb[0][j] = __builtin_nontemporal_load(vp + j * 8);
#pragma unroll
    for (int s = 1; s < NBUF; ++s) {
#pragma unroll
      for (int j = 0; j < 3; ++j) {
        kb[s][j] = __builtin_nontemporal_load(kp + s * 192 + j * 8);
        vb[s][j] = __builtin_nontemporal_load(vp + s * 192 + j * 8);
      }
    }
    __syncthreads();
    if (t < DH) {
      float s = 0.f;
#pragma unroll
      for (int g = 0; g < 20; ++g) s += red[g * DH + t];
      qs[t] = f2bf_bits(s);
    }
    __syncthreads();
    u32x4 qv[3];
#pragma unroll
    for (int j = 0; j < 3; ++j) qv[j] = *reinterpret_cast<const u32x4*>(qs + (sub + 8 * j) * 8);

    const float SC = 0.07216878364870322f * 1.4426950408889634f;
    float m = -INFINITY, l = 0.f;
    float acc[24];
#pragma unroll
    for (int i = 0; i < 24; ++i) acc[i] = 0.f;

    auto compute = [&](const u32x4 (&kk)[3], const u32x4 (&vv3)[3]) {
      float sc = 0.f;
#pragma unroll
      for (int j = 0; j < 3; ++j)
#pragma unroll
        for (int w = 0; w < 4; ++w) sc = dot2(kk[j][w], qv[j][w], sc);
      sc += dppf<0xB1>(sc);
      sc += dppf<0x4E>(sc);
      sc += dppf<0x141>(sc);
      sc *= SC;
      const float mn = fmaxf(m, sc);
      const float alpha = __builtin_amdgcn_exp2f(m - mn);
      const float p = __builtin_amdgcn_exp2f(sc - mn);
      l = fmaf(l, alpha, p);
      m = mn;
#pragma unroll
      for (int j = 0; j < 3; ++j)
#pragma unroll
        for (int w = 0; w < 4; ++w) {
          const u32 vv = vv3[j][w];
          acc[j * 8 + 2 * w] = fmaf(p, bf_lo(vv), acc[j * 8 + 2 * w] * alpha);
          acc[j * 8 + 2 * w + 1] = fmaf(p, bf_hi(vv), acc[j * 8 + 2 * w + 1] * alpha);
        }
    };
#pragma unroll 1
    for (int s0 = 0; s0 < NST - NBUF; s0 += NBUF) {
#pragma unroll
      for (int cur = 0; cur < NBUF; ++cur) {
        compute(kb[cur], vb[cur]);
#pragma unroll
        for (int j = 0; j < 3; ++j) {
          kb[cur][j] = __builtin_nontemporal_load(kp + (s0 + cur + NBUF) * 192 + j * 8);
          vb[cur][j] = __builtin_nontemporal_load(vp + (s0 + cur + NBUF) * 192 + j * 8);
        }
      }
    }
#pragma unroll
    for (int cur = 0; cur < NBUF; ++cur) compute(kb[cur], vb[cur]);

    float* mlm = lds + 64 * DH;
    float* mll = mlm + 64;
    float* buf = lds;
    const int slot = wave * 8 + rs;
    __syncthreads();
    if (sub == 0) { mlm[slot] = m; mll[slot] = l; }
    __syncthreads();
    float M = -INFINITY;
#pragma unroll 8
    for (int i = 0; i < 64; ++i) M = fmaxf(M, mlm[i]);
    float L = 0.f;
#pragma unroll 8
    for (int i = 0; i < 64; ++i) L += mll[i] * __builtin_amdgcn_exp2f(mlm[i] - M);
    const float msc = __builtin_amdgcn_exp2f(m - M);
#pragma unroll
    for (int j = 0; j < 3; ++j) {
      f32x4 o0, o1;
#pragma unroll
      for (int e = 0; e < 4; ++e) { o0[e] = acc[j * 8 + e] * msc; o1[e] = acc[j * 8 + 4 + e] * msc; }
      float* dst = buf + slot * DH + (sub + 8 * j) * 8;
      *reinterpret_cast<f32x4*>(dst) = o0;
      *reinterpret_cast<f32x4*>(dst + 4) = o1;
    }
    __syncthreads();
    float ov = 0.f;
    if (t < DH) {
#pragma unroll 8
      for (int i = 0; i < 64; ++i) ov += buf[i * DH + t];
      ov = rbf(ov / L);
    }
    __syncthreads();
    float* os = lds;
    float* red2 = lds + 256;
    if (t < DH) os[t] = ov;
    __syncthreads();

    if (t < 480) {
      const int cc = t % 80, rg = t / 80;
      float a[8];
#pragma unroll
      for (int e = 0; e < 8; ++e) a[e] = 0.f;
      const unsigned short* wrow2 = wo + (size_t)(h * DH + rg * 32) * DM + cc * 8;
#pragma unroll 8
      for (int i = 0; i < 32; ++i) {
        const u32x4 w = WLOAD(reinterpret_cast<const u32x4*>(wrow2 + (size_t)i * DM));
        const float o = os[rg * 32 + i];
#pragma unroll
        for (int e = 0; e < 4; ++e) {
          a[2 * e] = fmaf(o, bf_lo(w[e]), a[2 * e]);
          a[2 * e + 1] = fmaf(o, bf_hi(w[e]), a[2 * e + 1]);
        }
      }
#pragma unroll
      for (int e = 0; e < 8; ++e) red2[rg * DM + cc * 8 + e] = a[e];
    }
    __syncthreads();
    for (int n = t; n < DM; n += NTHR) {
      float s = 0.f;
#pragma unroll
      for (int g = 0; g < 6; ++g) s += red2[g * DM + n];
      att_part[(size_t)(b * HQ + h) * DM + n] = s;
    }
  } else {
    const int c = blockIdx.x - NATT;
    __builtin_amdgcn_s_setprio(3);
    float* xs = lds;
    float* red = lds + NB * DM;
    float* hs = red + 2 * 4 * 8 * 32;
    const int mat = wave >> 2, rgw = wave & 3;
    const int cc = lane & 3, rgl = lane >> 2;
    const int rg = rgw * 16 + rgl;
    const unsigned short* W = (mat ? wu : wg) + (size_t)c * FU + cc * 8;
    u32x4 w1r[10];
#pragma unroll
    for (int i = 0; i < 10; ++i) w1r[i] = WLOAD(reinterpret_cast<const u32x4*>(W + (size_t)(rg + 64 * i) * FF));
    const int ccl = lane & 15, ug = lane >> 4;
    const int cc2 = wave * 16 + ccl;
    u32x4 w2r[8];
    if (wave < 5) {
#pragma unroll
      for (int i = 0; i < 8; ++i) w2r[i] = WLOAD(reinterpret_cast<const u32x4*>(wd + (size_t)(c * FU + ug * 8 + i) * DM + cc2 * 8));
    }
    {
      u32x4 xv[2];
#pragma unroll
      for (int k = 0; k < 2; ++k) { const int ch = min(t + k * NTHR, NB * DM / 8 - 1); xv[k] = *reinterpret_cast<const u32x4*>(x + ch * 8); }
#pragma unroll
      for (int k = 0; k < 2; ++k) {
        const int ch = t + k * NTHR;
        if (ch < NB * DM / 8) {
          f32x4 lo, hi;
#pragma unroll
          for (int e = 0; e < 2; ++e) { lo[2 * e] = bf_lo(xv[k][e]); lo[2 * e + 1] = bf_hi(xv[k][e]); hi[2 * e] = bf_lo(xv[k][2 + e]); hi[2 * e + 1] = bf_hi(xv[k][2 + e]); }
          *reinterpret_cast<f32x4*>(xs + ch * 8) = lo;
          *reinterpret_cast<f32x4*>(xs + ch * 8 + 4) = hi;
        }
      }
    }
    __syncthreads();
#pragma unroll 1
    for (int bb = 0; bb < NB; ++bb) {
      float a[8];
#pragma unroll
      for (int e = 0; e < 8; ++e) a[e] = 0.f;
#pragma unroll
      for (int i = 0; i < 10; ++i) {
        asm volatile("" : "+v"(w1r[i]));
        const float xv = xs[bb * DM + rg + 64 * i];
#pragma unroll
        for (int e = 0; e < 4; ++e) {
          a[2 * e] = fmaf(xv, bf_lo(w1r[i][e]), a[2 * e]);
          a[2 * e + 1] = fmaf(xv, bf_hi(w1r[i][e]), a[2 * e + 1]);
        }
      }
#pragma unroll
      for (int e = 0; e < 8; ++e) {
        float v = a[e];
        v += __shfl_xor(v, 4);
        v += __shfl_xor(v, 8);
        v += __shfl_xor(v, 16);
        v += __shfl_xor(v, 32);
        a[e] = v;
      }
      if (lane < 4) {
#pragma unroll
        for (int e = 0; e < 8; ++e) red[((mat * 4 + rgw) * 8 + bb) * 32 + cc * 8 + e] = a[e];
      }
    }
    __syncthreads();
    if (t < NB * FU) {
      const int bb = t >> 5, u = t & 31;
      float g = 0.f, up = 0.f;
#pragma unroll
      for (int w = 0; w < 4; ++w) {
        g += red[((0 * 4 + w) * 8 + bb) * 32 + u];
        up += red[((1 * 4 + w) * 8 + bb) * 32 + u];
      }
      g = rbf(g);
      up = rbf(up);
      const float inner = 0.7978845608028654f * (g + 0.044715f * g * g * g);
      const float act = rbf(0.5f * g * (1.f + tanhf(inner)));
      hs[bb * FU + u] = rbf(act * up);
    }
    __syncthreads();
    if (wave < 5) {
#pragma unroll 1
      for (int bb = 0; bb < NB; ++bb) {
        float a[8];
#pragma unroll
        for (int e = 0; e < 8; ++e) a[e] = 0.f;
#pragma unroll
        for (int i = 0; i < 8; ++i) {
          asm volatile("" : "+v"(w2r[i]));
          const float hv = hs[bb * FU + ug * 8 + i];
#pragma unroll
          for (int e = 0; e < 4; ++e) {
            a[2 * e] = fmaf(hv, bf_lo(w2r[i][e]), a[2 * e]);
            a[2 * e + 1] = fmaf(hv, bf_hi(w2r[i][e]), a[2 * e + 1]);
          }
        }
#pragma unroll
        for (int e = 0; e < 8; ++e) {
          float v = a[e];
          v += __shfl_xor(v, 16);
          v += __shfl_xor(v, 32);
          a[e] = v;
        }
        if (lane < 16) {
          float* dst = ffn_part + ((size_t)c * NB + bb) * DM + cc2 * 8;
          *reinterpret_cast<f32x4*>(dst) = f32x4{a[0], a[1], a[2], a[3]};
          *reinterpret_cast<f32x4*>(dst + 4) = f32x4{a[4], a[5], a[6], a[7]};
        }
      }
    }
  }
}

__global__ void __launch_bounds__(256)
final_reduce(const unsigned short* __restrict__ x, const unsigned short* __restrict__ gw,
             const float* __restrict__ att_part, const float* __restrict__ ffn_part, unsigned short* __restrict__ out) {
  __shared__ float sa[4][64], sf[4][64], sz[4];
  const int b = blockIdx.x / 10, cg = blockIdx.x % 10, t = threadIdx.x;
  const int nl = t & 63, pg = t >> 6;
  const int n = cg * 64 + nl;
  float a = 0.f, f = 0.f;
  float av[8], fv[12];
#pragma unroll
  for (int i = 0; i < 8; ++i) av[i] = att_part[(size_t)(b * HQ + pg * 8 + i) * DM + n];
#pragma unroll
  for (int i = 0; i < 12; ++i) fv[i] = ffn_part[((size_t)(pg * 12 + i) * NB + b) * DM + n];
  float z = 0.f;
  for (int i = t; i < DM; i += 256) {
    const float xv = __builtin_bit_cast(float, (u32)x[b * DM + i] << 16);
    const float gv = __builtin_bit_cast(float, (u32)gw[i] << 16);
    z += rbf(xv * gv);
  }
#pragma unroll
  for (int i = 0; i < 8; ++i) a += av[i];
#pragma unroll
  for (int i = 0; i < 12; ++i) f += fv[i];
#pragma unroll
  for (int o = 32; o >= 1; o >>= 1) z += __shfl_xor(z, o);
  sa[pg][nl] = a; sf[pg][nl] = f;
  if (nl == 0) sz[pg] = z;
  __syncthreads();
  if (t < 64) {
    z = rbf(sz[0] + sz[1] + sz[2] + sz[3]);
    const float g = rbf(1.f / (1.f + __expf(-z)));
    a = rbf((sa[0][nl] + sa[1][nl]) + (sa[2][nl] + sa[3][nl]));
    f = rbf((sf[0][nl] + sf[1][nl]) + (sf[2][nl] + sf[3][nl]));
    const float omg = rbf(1.f - g);
    const float r = rbf(g * a) + rbf(omg * f);
    out[b * DM + n] = f2bf_bits(r);
  }
}

extern "C" void kernel_launch(void* const* d_in, const int* in_sizes, int n_in,
                              void* d_out, int out_size, void* d_ws, size_t ws_size,
                              hipStream_t stream) {
  const unsigned short* x = (const unsigned short*)d_in[0];
  const unsigned short* kc = (const unsigned short*)d_in[1];
  const unsigned short* vc = (const unsigned short*)d_in[2];
  const unsigned short* wq = (const unsigned short*)d_in[3];
  const unsigned short* wo = (const unsigned short*)d_in[4];
  const unsigned short* wg = (const unsigned short*)d_in[6];
  const unsigned short* wu = (const unsigned short*)d_in[7];
  const unsigned short* wd = (const unsigned short*)d_in[8];
  const unsigned short* gw = (const unsigned short*)d_in[9];
  float* att_part = (float*)((char*)d_ws + WS_ATT);
  float* ffn_part = (float*)((char*)d_ws + WS_FFN);
  constexpr int GR = NATT + NFFN;
  hipLaunchKernelGGL(fused_main, dim3(GR), dim3(NTHR), 0, stream, x, kc, vc, wq, wo, wg, wu, wd, att_part, ffn_part);
  hipLaunchKernelGGL(final_reduce, dim3(NB * 10), dim3(256), 0, stream, x, gw, (const float*)att_part, (const float*)ffn_part, (unsigned short*)d_out);
}
```
